```python
import torch

def _softmax(x, axis=-1):
    return torch.softmax(x, dim=axis)
def _gelu(x):
    return torch.nn.functional.gelu(x, approximate="tanh")
def _silu(x):
    return torch.nn.functional.silu(x)
def _sigmoid(x):
    return torch.sigmoid(x)
def _layernorm(x, axis=-1, eps=1e-5):
    m = x.mean(dim=axis, keepdim=True)
    v = x.var(dim=axis, keepdim=True, unbiased=False)
    return (x - m) / torch.sqrt(v + eps)
def _scatter_add(tgt, idx, src, axis=0):
    return tgt.clone().index_add_(axis, idx, src)
def _segment_sum(src, seg, n):
    out = torch.zeros((n,) + src.shape[1:], dtype=src.dtype)
    return out.index_add_(0, seg, src)
def _pad(x, pw):
    flat = tuple(v for pair in reversed(pw) for v in pair)
    return torch.nn.functional.pad(x, flat)

kernel_name = 'gated_mix__compressed_kv_cla__grouped_kv_mqa__prompt'

def setup_inputs():

    _w = (lambda a: (lambda t: t.to(torch.bfloat16) if t.is_floating_point() else t)(torch.as_tensor(a)))
    def _A_setup():
        B, H, Sq, Sk, D, M = 1, 32, 2048, 2048, 128, 512
        Dc = 4 * D
        return {
            "x": _w((lambda *s: torch.randn(*s, dtype=torch.bfloat16) * 0.05)(B, Sq, M)),
            "c_kv": _w((lambda *s: torch.randn(*s, dtype=torch.bfloat16) * 0.8)(B, Sk, Dc)),
            "w_q": _w((lambda *s: torch.randn(*s, dtype=torch.bfloat16) * 6.4)(M, H, D) * (M ** -0.5)),
            "w_uk": _w((lambda *s: torch.randn(*s, dtype=torch.bfloat16) * 6.4)(Dc, H, D) * (Dc ** -0.5)),
            "w_uv": _w((lambda *s: torch.randn(*s, dtype=torch.bfloat16) * 0.8)(Dc, H, D) * (Dc ** -0.5)),
            "w_o": _w((lambda *s: torch.randn(*s, dtype=torch.bfloat16) * 0.8)(H, D, M) * ((H * D) ** -0.5)),
        }
    _A_in = _A_setup()

    def _B_setup():
        B, S, D, H = 1, 2048, 512, 32
        Dh = D // H
        return {
            "x": _w((lambda *s: torch.randn(*s, dtype=torch.bfloat16) * 0.05)(B, S, D)),
            "wq": _w((lambda *s: torch.randn(*s, dtype=torch.bfloat16) * 3.2)(D, H * Dh)),
            "wk": _w((lambda *s: torch.randn(*s, dtype=torch.bfloat16) * 3.2)(D, Dh)),
            "wv": _w((lambda *s: torch.randn(*s, dtype=torch.bfloat16) * 0.1)(D, Dh)),
            "wo": _w((lambda *s: torch.randn(*s, dtype=torch.bfloat16) * 0.1)(H * Dh, D)),
        }
    _B_in = _B_setup()

    return {
        "A_x": _A_in["x"],
        "A_c_kv": _A_in["c_kv"],
        "A_w_q": _A_in["w_q"],
        "A_w_uk": _A_in["w_uk"],
        "A_w_uv": _A_in["w_uv"],
        "A_w_o": _A_in["w_o"],
        "B_x": _B_in["x"],
        "B_wq": _B_in["wq"],
        "B_wk": _B_in["wk"],
        "B_wv": _B_in["wv"],
        "B_wo": _B_in["wo"],
        "gate_w": _w((lambda *s: torch.randn(*s, dtype=torch.bfloat16) * 0.05)(512) * 16.0),
    }

def reference(A_x, A_c_kv, A_w_q, A_w_uk, A_w_uv, A_w_o, B_x, B_wq, B_wk, B_wv, B_wo, gate_w):
    def _A_ref(x, c_kv, w_q, w_uk, w_uv, w_o):
        B, Sq, M = x.shape[0], x.shape[1], x.shape[2]
        Sk = c_kv.shape[1]
        H, D = w_q.shape[1], w_q.shape[2]
        q = torch.einsum("bqm,mhd->bhqd", x, w_q)
        k = torch.einsum("bkc,chd->bhkd", c_kv, w_uk)
        v = torch.einsum("bkc,chd->bhkd", c_kv, w_uv)
        scores = torch.einsum("bhqd,bhkd->bhqk", q, k) * (D ** -0.5)
        off = Sk - Sq
        qi = torch.arange(Sq)[:, None]
        kj = torch.arange(Sk)[None, :]
        cmask = kj <= (qi + off)
        scores = torch.where(cmask[None, None], scores, -1e9)
        p = _softmax(scores, axis=-1)
        ctx = torch.einsum("bhqk,bhkd->bhqd", p, v)
        return torch.einsum("bhqd,hdm->bqm", ctx, w_o)

    def _B_ref(x, wq, wk, wv, wo):
        B, S, D = x.shape
        H = 32
        Dh = D // H
        q = (lambda x, *d: x.permute(*d))((x @ wq).reshape(B, S, H, Dh), 0, 2, 1, 3)
        k = x @ wk
        v = x @ wv
        scores = torch.einsum("bhqd,bkd->bhqk", q, k) * (Dh ** -0.5)
        p = _softmax(scores, axis=-1)
        o = torch.einsum("bhqk,bkd->bhqd", p, v)
        return (lambda x, *d: x.permute(*d))(o, 0, 2, 1, 3).reshape(B, S, H * Dh) @ wo

    x = A_x
    g = _sigmoid((x * gate_w).sum(-1, keepdims=True))
    a = _A_ref(x, A_c_kv, A_w_q, A_w_uk, A_w_uv, A_w_o)
    b = _B_ref(x, B_wq, B_wk, B_wv, B_wo)
    return g * a + (1 - g) * b
```

```cpp
#include <hip/hip_runtime.h>
#include <hip/hip_bf16.h>
#include <stdint.h>

typedef unsigned short u16;
typedef __attribute__((ext_vector_type(8))) short bf16x8;
typedef __attribute__((ext_vector_type(4))) short s16x4;
typedef __attribute__((ext_vector_type(16))) float f32x16;
typedef __attribute__((ext_vector_type(4))) float f32x4;
typedef __attribute__((ext_vector_type(2))) float f32x2;
typedef __attribute__((ext_vector_type(2))) __bf16 bf16x2_t;
typedef __attribute__((ext_vector_type(4))) unsigned u32x4;
typedef __attribute__((ext_vector_type(2))) unsigned u32x2;
typedef __attribute__((address_space(3))) s16x4 lds_s16x4;

#define DI __device__ __forceinline__

DI unsigned pk2(float lo, float hi) {
  f32x2 v = {lo, hi};
  bf16x2_t r = __builtin_convertvector(v, bf16x2_t);
  return __builtin_bit_cast(unsigned, r);
}
DI float bflo(unsigned u) { return __uint_as_float(u << 16); }
DI float bfhi(unsigned u) { return __uint_as_float(u & 0xffff0000u); }
DI float rbf(float x) { return bflo(pk2(x, 0.f)); }
DI s16x4 tr_read(const char* p) {
  return __builtin_amdgcn_ds_read_tr16_b64_v4i16((lds_s16x4*)(p));
}
DI bf16x8 cat8(s16x4 a, s16x4 b) { return __builtin_shufflevector(a, b, 0, 1, 2, 3, 4, 5, 6, 7); }
#define MFMA32(a, b, c) __builtin_amdgcn_mfma_f32_32x32x16_bf16((a), (b), (c), 0, 0, 0)

constexpr int SEQ = 2048, DM = 512, NH = 32, HDIM = 128, HD = 4096;

constexpr size_t WS_QA = 0;
constexpr size_t WS_KA = WS_QA + (size_t)SEQ * HD * 2;
constexpr size_t WS_VA = WS_KA + (size_t)SEQ * HD * 2;
constexpr size_t WS_CTX = WS_VA + (size_t)SEQ * HD * 2;
constexpr size_t WS_QB = WS_CTX + (size_t)SEQ * HD * 2;
constexpr size_t WS_OB = WS_QB + (size_t)SEQ * DM * 2;
constexpr size_t WS_KB = WS_OB + (size_t)SEQ * DM * 2;
constexpr size_t WS_VB = WS_KB + (size_t)SEQ * 16 * 2;
constexpr size_t WS_GATE = WS_VB + (size_t)SEQ * 16 * 2;
constexpr size_t WS_END = WS_GATE + (size_t)SEQ * 4;

constexpr int P_NT_MAIN = 3 * 16 * 32;
constexpr int P_NT_B = 16 * 5;
constexpr int P_NT_GATE = 128;
constexpr int P_GRID = P_NT_MAIN + P_NT_B + P_NT_GATE;

DI int a_off(int row, int ch) { return row * 128 + ((ch ^ ((row >> 1) & 7)) << 4); }
DI int w_off128(int k, int ch  ) { return (((k >> 2) * 4 + (ch >> 2)) << 8) + ((k & 3) << 6) + ((ch & 3) << 4); }

__global__ __launch_bounds__(256, 2) void proj_kernel(const u16* __restrict__ x, const u16* __restrict__ ckv,
                                                      const u16* __restrict__ wq, const u16* __restrict__ wuk,
                                                      const u16* __restrict__ wuv, const u16* __restrict__ bwq,
                                                      const u16* __restrict__ bwk, const u16* __restrict__ bwv,
                                                      const u16* __restrict__ gate_w,
                                                      u16* __restrict__ QA, u16* __restrict__ KA, u16* __restrict__ VA,
                                                      u16* __restrict__ QB, u16* __restrict__ KB, u16* __restrict__ VB,
                                                      float* __restrict__ gate) {
  __shared__ __attribute__((aligned(16))) char smem[2 * 32768];
  const int bid = blockIdx.x;
  const int tid = threadIdx.x, wid = tid >> 6, lane = tid & 63, r32 = lane & 31, hh = lane >> 5;

  if (bid >= P_NT_MAIN + P_NT_B) {
    const int gb = bid - (P_NT_MAIN + P_NT_B);
    const u32x4 gw = *(const u32x4*)(gate_w + lane * 8);
#pragma unroll
    for (int rr = 0; rr < 4; ++rr) {
      const int row = (gb * 4 + wid) * 4 + rr;
      const u32x4 xv = *(const u32x4*)(x + (size_t)row * DM + lane * 8);
      float s = 0.f;
#pragma unroll
      for (int j = 0; j < 4; ++j) {
        s += rbf(bflo(xv[j]) * bflo(gw[j]));
        s += rbf(bfhi(xv[j]) * bfhi(gw[j]));
      }
#pragma unroll
      for (int off = 32; off >= 1; off >>= 1) s += __shfl_xor(s, off);
      if (lane == 0) {
        const float sb = rbf(s);
        const float g = 1.f / (1.f + __expf(-sb));
        gate[row] = rbf(g);
      }
    }
    return;
  }

  const u16 *A, *W;
  u16* C;
  int ldw, tm, tn, kvtile = 0;
  if (bid < P_NT_MAIN) {
    const int job = bid >> 9, r = bid & 511;
    tm = r & 15; tn = r >> 4;
    A = job == 0 ? x : ckv;
    W = job == 0 ? wq : (job == 1 ? wuk : wuv);
    C = job == 0 ? QA : (job == 1 ? KA : VA);
    ldw = HD;
  } else {
    const int r = bid - P_NT_MAIN;
    tm = r & 15; tn = r >> 4;
    A = x; W = bwq; C = QB; ldw = DM;
    kvtile = (tn == 4);
  }
  const int m0 = tm * 128, n0 = tn * 128;

  u32x4 ra[4], rw[4];
  auto load_regs = [&](int k0) {
#pragma unroll
    for (int i = 0; i < 4; ++i) {
      const int id = tid + 256 * i;
      ra[i] = *(const u32x4*)(A + (size_t)(m0 + (id >> 3)) * DM + k0 + (id & 7) * 8);
    }
    if (!kvtile) {
#pragma unroll
      for (int i = 0; i < 4; ++i) {
        const int id = tid + 256 * i;
        rw[i] = *(const u32x4*)(W + (size_t)(k0 + (id >> 4)) * ldw + n0 + (id & 15) * 8);
      }
    } else {
#pragma unroll
      for (int i = 0; i < 4; ++i) {
        const int id = tid + 256 * i;
        const int kr = k0 + (id >> 4), ch = id & 15;
        u32x4 v = {0u, 0u, 0u, 0u};
        if (ch < 2) v = *(const u32x4*)(bwk + (size_t)kr * 16 + ch * 8);
        else if (ch < 4) v = *(const u32x4*)(bwv + (size_t)kr * 16 + (ch - 2) * 8);
        rw[i] = v;
      }
    }
  };
  auto write_lds = [&](int b) {
    char* sa = smem + b * 32768;
    char* sw = sa + 16384;
#pragma unroll
    for (int i = 0; i < 4; ++i) {
      const int id = tid + 256 * i;
      *(u32x4*)(sa + a_off(id >> 3, id & 7)) = ra[i];
    }
#pragma unroll
    for (int i = 0; i < 4; ++i) {
      const int id = tid + 256 * i;
      *(u32x4*)(sw + w_off128(id >> 4, id & 15)) = rw[i];
    }
  };

  const int wm = wid & 1, wn = wid >> 1;
  f32x16 acc[2][2];
#pragma unroll
  for (int a = 0; a < 2; ++a)
#pragma unroll
    for (int b = 0; b < 2; ++b)
#pragma unroll
      for (int i = 0; i < 16; ++i) acc[a][b][i] = 0.f;

  const int blk = (lane >> 4) & 1, q4 = (lane & 15) >> 2, p4 = lane & 3;
  const int w_lane = (hh * 2) * 1024 + q4 * 64 + blk * 32 + p4 * 8;
  int a_lane[2];
#pragma unroll
  for (int mb = 0; mb < 2; ++mb) a_lane[mb] = wm * 64 + mb * 32 + r32;

  load_regs(0);
  write_lds(0);
  __syncthreads();
  for (int kt = 0; kt < 8; ++kt) {
    if (kt + 1 < 8) load_regs((kt + 1) * 64);
    const char* sa = smem + (kt & 1) * 32768;
    const char* sw = sa + 16384;
#pragma unroll
    for (int ks = 0; ks < 4; ++ks) {
      bf16x8 wf[2], af[2];
#pragma unroll
      for (int cb = 0; cb < 2; ++cb) {
        const int c32 = wn * 2 + cb;
        const char* p = sw + w_lane + (4 * ks) * 1024 + c32 * 256;
        wf[cb] = cat8(tr_read(p), tr_read(p + 1024));
      }
#pragma unroll
      for (int mb = 0; mb < 2; ++mb) af[mb] = *(const bf16x8*)(sa + a_off(a_lane[mb], 2 * ks + hh));
#pragma unroll
      for (int cb = 0; cb < 2; ++cb)
#pragma unroll
        for (int mb = 0; mb < 2; ++mb) acc[cb][mb] = MFMA32(wf[cb], af[mb], acc[cb][mb]);
    }
    if (kt + 1 < 8) write_lds((kt + 1) & 1);
    __syncthreads();
  }

#pragma unroll
  for (int cb = 0; cb < 2; ++cb)
#pragma unroll
    for (int mb = 0; mb < 2; ++mb) {
      const int m = m0 + wm * 64 + mb * 32 + r32;
#pragma unroll
      for (int g = 0; g < 4; ++g) {
        const int nl = wn * 64 + cb * 32 + 8 * g + 4 * hh;
        u32x2 v;
        v[0] = pk2(acc[cb][mb][4 * g + 0], acc[cb][mb][4 * g + 1]);
        v[1] = pk2(acc[cb][mb][4 * g + 2], acc[cb][mb][4 * g + 3]);
        if (!kvtile) {
          *(u32x2*)(C + (size_t)m * ldw + n0 + nl) = v;
        } else {
          if (nl < 16) *(u32x2*)(KB + (size_t)m * 16 + nl) = v;
          else if (nl < 32) *(u32x2*)(VB + (size_t)m * 16 + (nl - 16)) = v;
        }
      }
    }
}

constexpr float LOG2E = 1.4426950408889634f;
constexpr float SCALE_A = 0.08838834764831845f;

DI void glds16(const void* g, void* l) {
  __builtin_amdgcn_global_load_lds((const __attribute__((address_space(1))) void*)g,
                                   (__attribute__((address_space(3))) void*)l, 16, 0, 0);
}

__global__ __launch_bounds__(512, 1) void attnA_kernel(const u16* __restrict__ Q, const u16* __restrict__ K,
                                                       const u16* __restrict__ V, u16* __restrict__ CTX) {
  extern __shared__ __attribute__((aligned(16))) char smem[];
  const int tid = threadIdx.x, wid = tid >> 6, lane = tid & 63, r32 = lane & 31, hh = lane >> 5;
  const int grp = wid >> 2, w4 = wid & 3;
  const int head = blockIdx.x & 31, pi = blockIdx.x >> 5;
  const u16* Qh = Q + head * HDIM;
  const u16* Kh = K + head * HDIM;
  const u16* Vh = V + head * HDIM;
  constexpr float CS = SCALE_A * LOG2E;

  const int blk = (lane >> 4) & 1, q4 = (lane & 15) >> 2, p4 = lane & 3;
  const int v_lane = hh * 1024 + q4 * 64 + blk * 32 + p4 * 8;
  int k_lane[8];
#pragma unroll
  for (int d0 = 0; d0 < 8; ++d0) k_lane[d0] = r32 * 256 + (((2 * d0 + hh) ^ (r32 & 15)) << 4);

  auto stage = [&](int st, int b) {
    char* kb = smem + b * 65536;
    char* vb = kb + 32768;
#pragma unroll
    for (int i = 0; i < 4; ++i) {
      const int j = wid * 4 + i;
      const int row = 4 * j + (lane >> 4);
      const int ch = (lane & 15) ^ (row & 15);
      glds16(Kh + (size_t)(st * 128 + row) * HD + ch * 8, kb + j * 1024);
    }
#pragma unroll
    for (int i = 0; i < 4; ++i) {
      const int j = wid * 4 + i;
      const int key = 4 * j + ((lane >> 2) & 3);
      glds16(Vh + (size_t)(st * 128 + key) * HD + (lane >> 4) * 32 + (lane & 3) * 8, vb + j * 1024);
    }
  };

  for (int half = 0; half < 2; ++half) {
    const int qb = half == 0 ? (15 - pi) : pi;
    const int R0 = qb * 128;
    const int qrow = R0 + 32 * w4 + r32;
    bf16x8 qr[8];
#pragma unroll
    for (int d0 = 0; d0 < 8; ++d0) qr[d0] = *(const bf16x8*)(Qh + (size_t)qrow * HD + d0 * 16 + hh * 8);

    float m_run = -1e30f, l_run = 0.f;
    f32x16 o[4];
#pragma unroll
    for (int c = 0; c < 4; ++c)
#pragma unroll
      for (int i = 0; i < 16; ++i) o[c][i] = 0.f;

    const int nst = qb + 1;
    stage(0, 0);
    __syncthreads();
    for (int st = 0; st < nst; ++st) {
      if (st + 1 < nst) stage(st + 1, (st + 1) & 1);
      const int key0 = (2 * st + grp) * 64;
      const bool active = key0 <= R0 + 32 * w4 + 31;
      if (active) {
        const char* kb = smem + (st & 1) * 65536 + grp * 16384;
        const char* vb = smem + (st & 1) * 65536 + 32768 + grp * 16384;
        f32x16 p0, p1;
#pragma unroll
        for (int i = 0; i < 16; ++i) { p0[i] = 0.f; p1[i] = 0.f; }
#pragma unroll
        for (int d0 = 0; d0 < 8; ++d0) {
          const bf16x8 k0 = *(const bf16x8*)(kb + k_lane[d0]);
          const bf16x8 k1 = *(const bf16x8*)(kb + k_lane[d0] + 8192);
          p0 = MFMA32(k0, qr[d0], p0);
          p1 = MFMA32(k1, qr[d0], p1);
        }
        if (key0 + 63 > R0 + 32 * w4) {
          const int dq = qrow - key0 - 4 * hh;
#pragma unroll
          for (int r = 0; r < 16; ++r) {
            const int c = (r & 3) + 8 * (r >> 2);
            if (c > dq) p0[r] = -INFINITY;
            if (c + 32 > dq) p1[r] = -INFINITY;
          }
        }
        float pmax = p0[0];
#pragma unroll
        for (int r = 1; r < 16; ++r) pmax = fmaxf(pmax, p0[r]);
#pragma unroll
        for (int r = 0; r < 16; ++r) pmax = fmaxf(pmax, p1[r]);
        pmax = fmaxf(pmax, __shfl_xor(pmax, 32));
        const float m_new = fmaxf(m_run, pmax);
        const float alpha = __builtin_amdgcn_exp2f((m_run - m_new) * CS);
        m_run = m_new;
        const float mc = -m_new * CS;
        float ps = 0.f;
#pragma unroll
        for (int r = 0; r < 16; ++r) { p0[r] = __builtin_amdgcn_exp2f(fmaf(p0[r], CS, mc)); ps += p0[r]; }
#pragma unroll
        for (int r = 0; r < 16; ++r) { p1[r] = __builtin_amdgcn_exp2f(fmaf(p1[r], CS, mc)); ps += p1[r]; }
        l_run = l_run * alpha + ps;
        if (__any(alpha < 1.f)) {
#pragma unroll
          for (int c = 0; c < 4; ++c)
#pragma unroll
            for (int i = 0; i < 16; ++i) o[c][i] *= alpha;
        }
        bf16x8 pf[4];
        {
          u32x4 t;
          t[0] = pk2(p0[0], p0[1]); t[1] = pk2(p0[2], p0[3]); t[2] = pk2(p0[4], p0[5]); t[3] = pk2(p0[6], p0[7]);
          pf[0] = __builtin_bit_cast(bf16x8, t);
          t[0] = pk2(p0[8], p0[9]); t[1] = pk2(p0[10], p0[11]); t[2] = pk2(p0[12], p0[13]); t[3] = pk2(p0[14], p0[15]);
          pf[1] = __builtin_bit_cast(bf16x8, t);
          t[0] = pk2(p1[0], p1[1]); t[1] = pk2(p1[2], p1[3]); t[2] = pk2(p1[4], p1[5]); t[3] = pk2(p1[6], p1[7]);
          pf[2] = __builtin_bit_cast(bf16x8, t);
          t[0] = pk2(p1[8], p1[9]); t[1] = pk2(p1[10], p1[11]); t[2] = pk2(p1[12], p1[13]); t[3] = pk2(p1[14], p1[15]);
          pf[3] = __builtin_bit_cast(bf16x8, t);
        }
#pragma unroll
        for (int c = 0; c < 4; ++c) {
#pragma unroll
          for (int s = 0; s < 4; ++s) {
            const char* p = vb + v_lane + (4 * s) * 1024 + c * 256;
            const bf16x8 vf = cat8(tr_read(p), tr_read(p + 2048));
            o[c] = MFMA32(vf, pf[s], o[c]);
          }
        }
      }
      __syncthreads();
    }

    float* mg = (float*)smem;
    if (grp == 1) {
      float* dst = mg + w4 * (66 * 64);
#pragma unroll
      for (int c = 0; c < 4; ++c)
#pragma unroll
        for (int i = 0; i < 16; ++i) dst[(c * 16 + i) * 64 + lane] = o[c][i];
      dst[64 * 64 + lane] = m_run;
      dst[65 * 64 + lane] = l_run;
    }
    __syncthreads();
    if (grp == 0) {
      const float* src = mg + w4 * (66 * 64);
      const float m1 = src[64 * 64 + lane], l1 = src[65 * 64 + lane];
      const float mm = fmaxf(m_run, m1);
      const float a0 = __builtin_amdgcn_exp2f((m_run - mm) * CS), a1 = __builtin_amdgcn_exp2f((m1 - mm) * CS);
      float lt = l_run * a0 + l1 * a1;
      lt += __shfl_xor(lt, 32);
      const float inv = 1.f / lt;
      u16* op = CTX + (size_t)qrow * HD + head * HDIM;
#pragma unroll
      for (int c = 0; c < 4; ++c) {
#pragma unroll
        for (int g = 0; g < 4; ++g) {
          float v[4];
#pragma unroll
          for (int e = 0; e < 4; ++e) v[e] = (o[c][4 * g + e] * a0 + src[(c * 16 + 4 * g + e) * 64 + lane] * a1) * inv;
          u32x2 w;
          w[0] = pk2(v[0], v[1]); w[1] = pk2(v[2], v[3]);
          *(u32x2*)(op + 32 * c + 8 * g + 4 * hh) = w;
        }
      }
    }
    __syncthreads();
  }
}

constexpr int VT_STRIDE = SEQ * 2 + 16;
constexpr int B_LDS = 2 * SEQ * 16 + 17 * VT_STRIDE;
constexpr float SCALE_B = 0.25f;

__global__ __launch_bounds__(512, 1) void attnB_kernel(const u16* __restrict__ QB, const u16* __restrict__ KB,
                                                       const u16* __restrict__ VB, u16* __restrict__ OB) {
  extern __shared__ __attribute__((aligned(16))) char smem[];
  char* sk = smem;
  char* sv = smem + 2 * SEQ * 16;
  const int tid = threadIdx.x, wid = tid >> 6, lane = tid & 63, r32 = lane & 31, hh = lane >> 5;
  constexpr float CS = SCALE_B * LOG2E;
  for (int i = tid; i < SEQ * 2; i += 512) {
    const int key = i >> 1, h = i & 1;
    *(u32x4*)(sk + h * (SEQ * 16) + key * 16) = *(const u32x4*)(KB + (size_t)key * 16 + h * 8);
  }
  for (int i = tid; i < SEQ * 2; i += 512) {
    const int key = i >> 1, h = i & 1;
    const u32x4 v = *(const u32x4*)(VB + (size_t)key * 16 + h * 8);
    const int pos = (key & ~12) | ((key & 4) << 1) | ((key & 8) >> 1);
#pragma unroll
    for (int j = 0; j < 4; ++j) {
      *(u16*)(sv + (h * 8 + 2 * j) * VT_STRIDE + pos * 2) = (u16)(v[j] & 0xffffu);
      *(u16*)(sv + (h * 8 + 2 * j + 1) * VT_STRIDE + pos * 2) = (u16)(v[j] >> 16);
    }
  }
  for (int i = tid; i < SEQ / 2; i += 512) *(unsigned*)(sv + 16 * VT_STRIDE + i * 4) = 0x3f803f80u;
  __syncthreads();

  const int gw = blockIdx.x * 8 + wid;
  const int head = gw & 31, rb = gw >> 5;
  const int q = rb * 32 + r32;
  const bf16x8 qf = *(const bf16x8*)(QB + (size_t)q * DM + head * 16 + hh * 8);
  const char* kp = sk + hh * (SEQ * 16) + r32 * 16;
  const int vrow = r32 < 16 ? r32 : 16;
  const char* vp = sv + vrow * VT_STRIDE + hh * 16;

  f32x16 z;
#pragma unroll
  for (int i = 0; i < 16; ++i) z[i] = 0.f;
  float mx = -INFINITY;
  for (int T = 0; T < SEQ / 32; ++T) {
    const bf16x8 kf = *(const bf16x8*)(kp + T * 512);
    const f32x16 s = MFMA32(kf, qf, z);
#pragma unroll
    for (int r = 0; r < 16; ++r) mx = fmaxf(mx, s[r]);
  }
  mx = fmaxf(mx, __shfl_xor(mx, 32));
  const float mc = -mx * CS;
  f32x16 o;
#pragma unroll
  for (int i = 0; i < 16; ++i) o[i] = 0.f;
  for (int T = 0; T < SEQ / 32; ++T) {
    const bf16x8 kf = *(const bf16x8*)(kp + T * 512);
    f32x16 s = MFMA32(kf, qf, z);
#pragma unroll
    for (int r = 0; r < 16; ++r) s[r] = __builtin_amdgcn_exp2f(fmaf(s[r], CS, mc));
    u32x4 t0, t1;
    t0[0] = pk2(s[0], s[1]); t0[1] = pk2(s[2], s[3]); t0[2] = pk2(s[4], s[5]); t0[3] = pk2(s[6], s[7]);
    t1[0] = pk2(s[8], s[9]); t1[1] = pk2(s[10], s[11]); t1[2] = pk2(s[12], s[13]); t1[3] = pk2(s[14], s[15]);
    const bf16x8 v0 = *(const bf16x8*)(vp + T * 64);
    const bf16x8 v1 = *(const bf16x8*)(vp + T * 64 + 32);
    o = MFMA32(v0, __builtin_bit_cast(bf16x8, t0), o);
    o = MFMA32(v1, __builtin_bit_cast(bf16x8, t1), o);
  }
  const float inv = 1.f / o[8];
  u16* op = OB + (size_t)q * DM + head * 16;
  u32x2 w0, w1;
  w0[0] = pk2(o[0] * inv, o[1] * inv); w0[1] = pk2(o[2] * inv, o[3] * inv);
  w1[0] = pk2(o[4] * inv, o[5] * inv); w1[1] = pk2(o[6] * inv, o[7] * inv);
  *(u32x2*)(op + 4 * hh) = w0;
  *(u32x2*)(op + 8 + 4 * hh) = w1;
}

DI int w_off64(int k, int ch  ) { return (((k >> 2) * 2 + (ch >> 2)) << 8) + ((k & 3) << 6) + ((ch & 3) << 4); }

__global__ __launch_bounds__(512, 1) void outproj_kernel(const u16* __restrict__ CTX, const u16* __restrict__ wo,
                                                         const u16* __restrict__ OB, const u16* __restrict__ bwo,
                                                         const float* __restrict__ gate, u16* __restrict__ out) {
  __shared__ __attribute__((aligned(16))) char smem[2 * 2 * 16384];
  const int tid = threadIdx.x, wid = tid >> 6, lane = tid & 63, r32 = lane & 31, hh = lane >> 5;
  const int grp = wid >> 2, w4 = wid & 3, wm = w4 & 1, wn = w4 >> 1;
  const int tm = blockIdx.x >> 3, tn = blockIdx.x & 7;
  const int m0 = tm * 64, n0 = tn * 64;
  const int gt = tid & 255;

  u32x4 ra[2], rw[2];
  auto load_regs = [&](const u16* A, int lda, const u16* W, int k0) {
#pragma unroll
    for (int i = 0; i < 2; ++i) {
      const int id = gt + 256 * i;
      ra[i] = *(const u32x4*)(A + (size_t)(m0 + (id >> 3)) * lda + k0 + (id & 7) * 8);
      rw[i] = *(const u32x4*)(W + (size_t)(k0 + (id >> 3)) * DM + n0 + (id & 7) * 8);
    }
  };
  auto write_lds = [&](int b) {
    char* sa = smem + (b * 2 + grp) * 16384;
    char* sw = sa + 8192;
#pragma unroll
    for (int i = 0; i < 2; ++i) {
      const int id = gt + 256 * i;
      *(u32x4*)(sa + a_off(id >> 3, id & 7)) = ra[i];
      *(u32x4*)(sw + w_off64(id >> 3, id & 7)) = rw[i];
    }
  };
  const int blk = (lane >> 4) & 1, q4 = (lane & 15) >> 2, p4 = lane & 3;
  const int w_lane = (hh * 2) * 512 + wn * 256 + q4 * 64 + blk * 32 + p4 * 8;
  const int a_row = wm * 32 + r32;

  f32x16 acc_a, acc_b;
#pragma unroll
  for (int i = 0; i < 16; ++i) { acc_a[i] = 0.f; acc_b[i] = 0.f; }

  const int nsteps = 32 + 4;
  auto src = [&](int step, const u16*& A, int& lda, const u16*& W, int& k0) {
    if (step < 32) { A = CTX; lda = HD; W = wo; k0 = grp * 2048 + step * 64; }
    else { A = OB; lda = DM; W = bwo; k0 = grp * 256 + (step - 32) * 64; }
  };
  {
    const u16 *A, *W; int lda, k0;
    src(0, A, lda, W, k0);
    load_regs(A, lda, W, k0);
    write_lds(0);
  }
  __syncthreads();
  for (int step = 0; step < nsteps; ++step) {
    if (step + 1 < nsteps) {
      const u16 *A, *W; int lda, k0;
      src(step + 1, A, lda, W, k0);
      load_regs(A, lda, W, k0);
    }
    const char* sa = smem + ((step & 1) * 2 + grp) * 16384;
    const char* sw = sa + 8192;
    if (step < 32) {
#pragma unroll
      for (int ks = 0; ks < 4; ++ks) {
        const char* p = sw + w_lane + (4 * ks) * 512;
        const bf16x8 wf = cat8(tr_read(p), tr_read(p + 512));
        const bf16x8 af = *(const bf16x8*)(sa + a_off(a_row, 2 * ks + hh));
        acc_a = MFMA32(wf, af, acc_a);
      }
    } else {
#pragma unroll
      for (int ks = 0; ks < 4; ++ks) {
        const char* p = sw + w_lane + (4 * ks) * 512;
        const bf16x8 wf = cat8(tr_read(p), tr_read(p + 512));
        const bf16x8 af = *(const bf16x8*)(sa + a_off(a_row, 2 * ks + hh));
        acc_b = MFMA32(wf, af, acc_b);
      }
    }
    if (step + 1 < nsteps) write_lds((step + 1) & 1);
    __syncthreads();
  }
  float* mg = (float*)smem;
  if (grp == 1) {
    float* dst = mg + w4 * (32 * 64);
#pragma unroll
    for (int i = 0; i < 16; ++i) { dst[i * 64 + lane] = acc_a[i]; dst[(16 + i) * 64 + lane] = acc_b[i]; }
  }
  __syncthreads();
  if (grp == 0) {
    const float* s = mg + w4 * (32 * 64);
    const int m = m0 + wm * 32 + r32;
    const float g = gate[m];
    const float omg = rbf(1.f - g);
#pragma unroll
    for (int gq = 0; gq < 4; ++gq) {
      float v[4];
#pragma unroll
      for (int e = 0; e < 4; ++e) {
        const int i = 4 * gq + e;
        const float a = rbf(acc_a[i] + s[i * 64 + lane]);
        const float b = rbf(acc_b[i] + s[(16 + i) * 64 + lane]);
        v[e] = rbf(g * a) + rbf(omg * b);
      }
      u32x2 w;
      w[0] = pk2(v[0], v[1]); w[1] = pk2(v[2], v[3]);
      *(u32x2*)(out + (size_t)m * DM + n0 + wn * 32 + 8 * gq + 4 * hh) = w;
    }
  }
}

extern "C" void kernel_launch(void* const* d_in, const int* in_sizes, int n_in,
                              void* d_out, int out_size, void* d_ws, size_t ws_size,
                              hipStream_t stream) {
  const u16* A_x = (const u16*)d_in[0];
  const u16* A_ckv = (const u16*)d_in[1];
  const u16* A_wq = (const u16*)d_in[2];
  const u16* A_wuk = (const u16*)d_in[3];
  const u16* A_wuv = (const u16*)d_in[4];
  const u16* A_wo = (const u16*)d_in[5];
  const u16* B_wq = (const u16*)d_in[7];
  const u16* B_wk = (const u16*)d_in[8];
  const u16* B_wv = (const u16*)d_in[9];
  const u16* B_wo = (const u16*)d_in[10];
  const u16* gate_w = (const u16*)d_in[11];
  char* ws = (char*)d_ws;
  u16* QA = (u16*)(ws + WS_QA);
  u16* KA = (u16*)(ws + WS_KA);
  u16* VA = (u16*)(ws + WS_VA);
  u16* CTX = (u16*)(ws + WS_CTX);
  u16* QB = (u16*)(ws + WS_QB);
  u16* OB = (u16*)(ws + WS_OB);
  u16* KB = (u16*)(ws + WS_KB);
  u16* VB = (u16*)(ws + WS_VB);
  float* gate = (float*)(ws + WS_GATE);

  hipFuncSetAttribute((const void*)attnA_kernel, hipFuncAttributeMaxDynamicSharedMemorySize, 131072);
  hipFuncSetAttribute((const void*)attnB_kernel, hipFuncAttributeMaxDynamicSharedMemorySize, B_LDS);

  proj_kernel<<<P_GRID, 256, 0, stream>>>(A_x, A_ckv, A_wq, A_wuk, A_wuv, B_wq, B_wk, B_wv, gate_w,
                                          QA, KA, VA, QB, KB, VB, gate);
  attnA_kernel<<<256, 512, 131072, stream>>>(QA, KA, VA, CTX);
  attnB_kernel<<<256, 512, B_LDS, stream>>>(QB, KB, VB, OB);
  outproj_kernel<<<256, 512, 0, stream>>>(CTX, A_wo, OB, B_wo, gate, (u16*)d_out);
}
```

```cpp
#include <hip/hip_runtime.h>
#include <hip/hip_bf16.h>
#include <stdint.h>

typedef unsigned short u16;
typedef __attribute__((ext_vector_type(8))) short bf16x8;
typedef __attribute__((ext_vector_type(4))) short s16x4;
typedef __attribute__((ext_vector_type(16))) float f32x16;
typedef __attribute__((ext_vector_type(4))) float f32x4;
typedef __attribute__((ext_vector_type(2))) float f32x2;
typedef __attribute__((ext_vector_type(2))) __bf16 bf16x2_t;
typedef __attribute__((ext_vector_type(4))) unsigned u32x4;
typedef __attribute__((ext_vector_type(2))) unsigned u32x2;
typedef __attribute__((address_space(3))) s16x4 lds_s16x4;

#define DI __device__ __forceinline__

DI unsigned pk2(float lo, float hi) {
  f32x2 v = {lo, hi};
  bf16x2_t r = __builtin_convertvector(v, bf16x2_t);
  return __builtin_bit_cast(unsigned, r);
}
DI float bflo(unsigned u) { return __uint_as_float(u << 16); }
DI float bfhi(unsigned u) { return __uint_as_float(u & 0xffff0000u); }
DI float rbf(float x) { return bflo(pk2(x, 0.f)); }
DI s16x4 tr_read(const char* p) {
  return __builtin_amdgcn_ds_read_tr16_b64_v4i16((lds_s16x4*)(p));
}
DI bf16x8 cat8(s16x4 a, s16x4 b) { return __builtin_shufflevector(a, b, 0, 1, 2, 3, 4, 5, 6, 7); }
#define MFMA32(a, b, c) __builtin_amdgcn_mfma_f32_32x32x16_bf16((a), (b), (c), 0, 0, 0)

constexpr int SEQ = 2048, DM = 512, NH = 32, HDIM = 128, HD = 4096;

constexpr size_t WS_QA = 0;
constexpr size_t WS_KA = WS_QA + (size_t)SEQ * HD * 2;
constexpr size_t WS_VA = WS_KA + (size_t)SEQ * HD * 2;
constexpr size_t WS_CTX = WS_VA + (size_t)SEQ * HD * 2;
constexpr size_t WS_QB = WS_CTX + (size_t)SEQ * HD * 2;
constexpr size_t WS_OB = WS_QB + (size_t)SEQ * DM * 2;
constexpr size_t WS_KB = WS_OB + (size_t)SEQ * DM * 2;
constexpr size_t WS_VB = WS_KB + (size_t)SEQ * 16 * 2;
constexpr size_t WS_GATE = WS_VB + (size_t)SEQ * 16 * 2;
constexpr size_t WS_END = WS_GATE + (size_t)SEQ * 4;

DI void glds16a(const void* gsrc, unsigned lds_dst) {
  unsigned keep;
  const unsigned lds_dst_uniform = __builtin_amdgcn_readfirstlane(lds_dst);
  asm volatile("s_mov_b32 %0, m0\n\ts_mov_b32 m0, %2\n\ts_nop 0\n\tglobal_load_lds_dwordx4 %1, off\n\ts_mov_b32 m0, %0"
               : "=&s"(keep) : "v"(gsrc), "s"(lds_dst_uniform) : "memory");
}
DI void glds16s(const void* sbase_ptr, unsigned voff, unsigned lds_dst) {
  unsigned keep;
  const unsigned lds_dst_uniform = __builtin_amdgcn_readfirstlane(lds_dst);
  const unsigned long long a = (unsigned long long)sbase_ptr;
  const unsigned alo = __builtin_amdgcn_readfirstlane((unsigned)a), ahi = __builtin_amdgcn_readfirstlane((unsigned)(a >> 32));
  const unsigned long long au = ((unsigned long long)ahi << 32) | alo;
  asm volatile("s_mov_b32 %0, m0\n\ts_mov_b32 m0, %3\n\ts_nop 0\n\tglobal_load_lds_dwordx4 %1, %2\n\ts_mov_b32 m0, %0"
               : "=&s"(keep) : "v"(voff), "s"(au), "s"(lds_dst_uniform) : "memory");
}
DI unsigned lds_addr(const void* p) { return (unsigned)(size_t)(__attribute__((address_space(3))) const char*)p; }
#define WAIT_VM(n) asm volatile("s_waitcnt vmcnt(" #n ")" ::: "memory")
#define WAIT_LGKM0() asm volatile("s_waitcnt lgkmcnt(0)" ::: "memory")
#define BARRIER() __builtin_amdgcn_s_barrier()

DI int a_off(int row, int ch) { return row * 128 + ((ch ^ ((row >> 1) & 7)) << 4); }

constexpr int PJ_STAGE = 32768 + 16384;
constexpr int PJ_LDS = 3 * PJ_STAGE;


__global__ __launch_bounds__(512, 1) void proj_kernel(const u16* __restrict__ x, const u16* __restrict__ ckv,
                                                      const u16* __restrict__ wq, const u16* __restrict__ wuk,
                                                      const u16* __restrict__ wuv, const u16* __restrict__ bwq,
                                                      const u16* __restrict__ bwk, const u16* __restrict__ bwv,
                                                      const u16* __restrict__ gate_w,
                                                      u16* __restrict__ QA, u16* __restrict__ KA, u16* __restrict__ VA,
                                                      u16* __restrict__ QB, u16* __restrict__ KB, u16* __restrict__ VB,
                                                      float* __restrict__ gate) {
  extern __shared__ __attribute__((aligned(16))) char smem[];
  const int bid = blockIdx.x;
  const int tid = threadIdx.x, lane = tid & 63, r32 = lane & 31, hh = lane >> 5;
  const int wid = __builtin_amdgcn_readfirstlane(tid >> 6);
  const unsigned sbase = lds_addr(smem);

  {
    const u32x4 gw = *(const u32x4*)(gate_w + lane * 8);
    const int row = bid * 8 + wid;
    const u32x4 xv = *(const u32x4*)(x + (size_t)row * DM + lane * 8);
    float s = 0.f;
#pragma unroll
    for (int j = 0; j < 4; ++j) {
      s += rbf(bflo(xv[j]) * bflo(gw[j]));
      s += rbf(bfhi(xv[j]) * bfhi(gw[j]));
    }
#pragma unroll
    for (int off = 32; off >= 1; off >>= 1) s += __shfl_xor(s, off);
    if (lane == 0) {
      const float sb = rbf(s);
      const float g = 1.f / (1.f + __expf(-sb));
      gate[row] = rbf(g);
    }
  }

  const int ntiles = bid < 40 ? 4 : 3;
  const int nsteps = ntiles * 8;
  const int q_n0 = ((bid & 7) * 4 + ((bid >> 3) & 3)) * 128, q_m0 = (bid >> 5) * 256;
  const int b_m0 = (bid & 7) * 256, b_tn = bid >> 3;
#define T_A(t) ((t) == 0 || (t) == 3 ? x : ckv)
#define T_W(t) ((t) == 0 ? wq : (t) == 1 ? wuk : (t) == 2 ? wuv : bwq)
#define T_C(t) ((t) == 0 ? QA : (t) == 1 ? KA : (t) == 2 ? VA : QB)
#define T_LDW(t) ((t) < 3 ? HD : DM)
#define T_M0(t) ((t) < 3 ? q_m0 : b_m0)
#define T_N0(t) ((t) < 3 ? q_n0 : b_tn * 128)
#define T_KV(t) ((t) == 3 && b_tn == 4)

  const int a_rowl = lane >> 3;
  const int a_ch0 = (lane & 7) ^ ((lane >> 4) & 7);
  const int a_ch1 = (lane & 7) ^ ((4 + (lane >> 4)) & 7);
  const int w_c = lane >> 4, w_kr = (lane >> 2) & 3, w_c4 = lane & 3;

  unsigned voffA[4], voffW[2];
#pragma unroll
  for (int i = 0; i < 4; ++i) {
    const int row = 8 * (wid * 4 + i) + a_rowl;
    voffA[i] = (unsigned)(row * DM + (((i & 1) ? a_ch1 : a_ch0) * 8)) * 2u;
  }
  const char* st_A = nullptr; const char* st_W = nullptr;
  int st_s = 0;
#define PJ_SET_TILE(t_) do {                                                                                     \
    st_A = (const char*)(T_A(t_) + (size_t)T_M0(t_) * DM);                                                       \
    if (!T_KV(t_)) {                                                                                             \
      st_W = (const char*)(T_W(t_) + T_N0(t_));                                                                  \
      _Pragma("unroll") for (int i_ = 0; i_ < 2; ++i_)                                                           \
        voffW[i_] = (unsigned)((4 * (wid * 2 + i_) + w_kr) * T_LDW(t_) + 32 * w_c + 8 * w_c4) * 2u;              \
    }                                                                                                            \
  } while (0)
#define PJ_STAGE_ISSUE() do {                                                                                    \
    const int kt_ = st_s & 7;                                                                                    \
    if (kt_ == 0) PJ_SET_TILE(st_s >> 3);                                                                        \
    const unsigned lb_ = sbase + (st_s % 3) * PJ_STAGE;                                                          \
    _Pragma("unroll") for (int i_ = 0; i_ < 4; ++i_) glds16s(st_A, voffA[i_], lb_ + (wid * 4 + i_) * 1024);      \
    if (!T_KV(st_s >> 3)) {                                                                                      \
      _Pragma("unroll") for (int i_ = 0; i_ < 2; ++i_) glds16s(st_W, voffW[i_], lb_ + 32768 + (wid * 2 + i_) * 1024); \
      st_W += (size_t)64 * T_LDW(st_s >> 3) * 2;                                                                 \
    } else {                                                                                                     \
      _Pragma("unroll") for (int i_ = 0; i_ < 2; ++i_) {                                                         \
        const int k_ = kt_ * 64 + 4 * (wid * 2 + i_) + w_kr;                                                     \
        const u16* src_ = (w_c4 < 2 ? bwk + (size_t)k_ * 16 + w_c4 * 8 : bwv + (size_t)k_ * 16 + (w_c4 - 2) * 8); \
        glds16a(src_, lb_ + 32768 + (wid * 2 + i_) * 1024);                                                      \
      }                                                                                                          \
    }                                                                                                            \
    st_A += 128;                                                                                                 \
    ++st_s;                                                                                                      \
  } while (0)

  const int wm = wid & 3, wn = wid >> 2;
  const int blk = (lane >> 4) & 1, q4 = (lane & 15) >> 2, p4 = lane & 3;
  const int w_lane = (hh * 2) * 1024 + q4 * 64 + blk * 32 + p4 * 8;
  int a_lane[2];
#pragma unroll
  for (int mb = 0; mb < 2; ++mb) a_lane[mb] = wm * 64 + mb * 32 + r32;

  f32x16 acc[2][2];
#pragma unroll
  for (int a = 0; a < 2; ++a)
#pragma unroll
    for (int b = 0; b < 2; ++b)
#pragma unroll
      for (int i = 0; i < 16; ++i) acc[a][b][i] = 0.f;

  PJ_STAGE_ISSUE();
  PJ_STAGE_ISSUE();
  for (int s = 0; s < nsteps; ++s) {
    if (s + 1 < nsteps) { WAIT_VM(6); } else { WAIT_VM(0); }
    WAIT_LGKM0();
    BARRIER();
    const char* sa = smem + (s % 3) * PJ_STAGE;
    const char* sw = sa + 32768;
    bf16x8 wf[2][2], af[2][2];
#define PJ_LOADF(set_, ks_) do {                                                                                  \
      _Pragma("unroll") for (int cb = 0; cb < 2; ++cb) {                                                          \
        const char* p_ = sw + w_lane + (4 * (ks_)) * 1024 + (wn * 2 + cb) * 256;                                  \
        wf[set_][cb] = cat8(tr_read(p_), tr_read(p_ + 1024));                                                     \
      }                                                                                                           \
      _Pragma("unroll") for (int mb = 0; mb < 2; ++mb) af[set_][mb] = *(const bf16x8*)(sa + a_off(a_lane[mb], 2 * (ks_) + hh)); \
    } while (0)
#define PJ_MMA(set_) do {                                                                                         \
      _Pragma("unroll") for (int cb = 0; cb < 2; ++cb)                                                            \
        _Pragma("unroll") for (int mb = 0; mb < 2; ++mb) acc[cb][mb] = MFMA32(wf[set_][cb], af[set_][mb], acc[cb][mb]); \
    } while (0)
    PJ_LOADF(0, 0);
    __builtin_amdgcn_sched_barrier(0);
    PJ_LOADF(1, 1);
    __builtin_amdgcn_sched_barrier(0);
    PJ_MMA(0);
    __builtin_amdgcn_sched_barrier(0);
    if (s + 2 < nsteps) PJ_STAGE_ISSUE();
    __builtin_amdgcn_sched_barrier(0);
    PJ_LOADF(0, 2);
    __builtin_amdgcn_sched_barrier(0);
    PJ_MMA(1);
    __builtin_amdgcn_sched_barrier(0);
    PJ_LOADF(1, 3);
    __builtin_amdgcn_sched_barrier(0);
    PJ_MMA(0);
    __builtin_amdgcn_sched_barrier(0);
    PJ_MMA(1);
    if ((s & 7) == 7) {
      const int t = s >> 3;
      u16* pC = T_C(t); const int pldw = T_LDW(t), pm0 = T_M0(t), pn0 = T_N0(t); const bool pkv = T_KV(t);
      if (!pkv) {
        WAIT_LGKM0();
        BARRIER();
        char* ep = smem + (s % 3) * PJ_STAGE + wid * 6144;
#pragma unroll
        for (int mb = 0; mb < 2; ++mb) {
#pragma unroll
          for (int cb = 0; cb < 2; ++cb)
#pragma unroll
            for (int g = 0; g < 4; ++g) {
              u32x2 v;
              v[0] = pk2(acc[cb][mb][4 * g + 0], acc[cb][mb][4 * g + 1]);
              v[1] = pk2(acc[cb][mb][4 * g + 2], acc[cb][mb][4 * g + 3]);
              *(u32x2*)(ep + r32 * 144 + (32 * cb + 8 * g + 4 * hh) * 2) = v;
            }
#pragma unroll
          for (int it = 0; it < 4; ++it) {
            const int row = (lane >> 3) + 8 * it;
            const u32x4 v = *(const u32x4*)(ep + row * 144 + (lane & 7) * 16);
            *(u32x4*)(pC + (size_t)(pm0 + wm * 64 + mb * 32 + row) * pldw + pn0 + wn * 64 + (lane & 7) * 8) = v;
          }
        }
      } else {
#pragma unroll
        for (int mb = 0; mb < 2; ++mb) {
          const int m = pm0 + wm * 64 + mb * 32 + r32;
          if (wn == 0) {
#pragma unroll
            for (int g = 0; g < 4; ++g) {
              const int nl = 8 * g + 4 * hh;
              u32x2 v;
              v[0] = pk2(acc[0][mb][4 * g + 0], acc[0][mb][4 * g + 1]);
              v[1] = pk2(acc[0][mb][4 * g + 2], acc[0][mb][4 * g + 3]);
              if (nl < 16) *(u32x2*)(KB + (size_t)m * 16 + nl) = v;
              else *(u32x2*)(VB + (size_t)m * 16 + (nl - 16)) = v;
            }
          }
        }
      }
#pragma unroll
      for (int cb = 0; cb < 2; ++cb)
#pragma unroll
        for (int mb = 0; mb < 2; ++mb)
#pragma unroll
          for (int i = 0; i < 16; ++i) acc[cb][mb][i] = 0.f;
    }
  }
}

constexpr float LOG2E = 1.4426950408889634f;
constexpr float SCALE_A = 0.08838834764831845f;
constexpr int AA_LDS = 163840;

__global__ __launch_bounds__(512, 1) void attnA_kernel(const u16* __restrict__ Q, const u16* __restrict__ K,
                                                       const u16* __restrict__ V, u16* __restrict__ CTX) {
  extern __shared__ __attribute__((aligned(16))) char smem[];
  const int tid = threadIdx.x, lane = tid & 63, r32 = lane & 31, hh = lane >> 5;
  const int wid = __builtin_amdgcn_readfirstlane(tid >> 6);
  const int grp = wid >> 2, w4 = wid & 3;
  const int head = blockIdx.x & 31, pi = blockIdx.x >> 5;
  const u16* Qh = Q + head * HDIM;
  const char* Kh = (const char*)(K + head * HDIM);
  const char* Vh = (const char*)(V + head * HDIM);
  constexpr float CS = SCALE_A * LOG2E;
  const unsigned sbase = lds_addr(smem);

  const int blk = (lane >> 4) & 1, q4 = (lane & 15) >> 2, p4 = lane & 3;
  const int v_lane = hh * 1024 + q4 * 64 + blk * 32 + p4 * 8;
  int k_lane[8];
#pragma unroll
  for (int d0 = 0; d0 < 8; ++d0) k_lane[d0] = r32 * 256 + (((2 * d0 + hh) ^ (r32 & 15)) << 4);
  unsigned voffK[4], voffV[4];
#pragma unroll
  for (int i = 0; i < 4; ++i) {
    const int j = wid * 4 + i;
    const int row = 4 * j + (lane >> 4);
    voffK[i] = (unsigned)(row * HD + (((lane & 15) ^ (row & 15)) * 8)) * 2u;
    const int key = 4 * j + ((lane >> 2) & 3);
    voffV[i] = (unsigned)(key * HD + (lane >> 4) * 32 + (lane & 3) * 8) * 2u;
  }
#define A_STAGE(st_) do {                                                                                        \
    const char* kp_ = Kh + (size_t)(st_) * 128 * HD * 2;                                                         \
    const char* vp_ = Vh + (size_t)(st_) * 128 * HD * 2;                                                         \
    const unsigned lk_ = sbase + ((st_) & 1) * 32768 + wid * 4096;                                               \
    const unsigned lv_ = sbase + 65536 + ((st_) % 3) * 32768 + wid * 4096;                                       \
    _Pragma("unroll") for (int i_ = 0; i_ < 4; ++i_) glds16s(kp_, voffK[i_], lk_ + i_ * 1024);                   \
    _Pragma("unroll") for (int i_ = 0; i_ < 4; ++i_) glds16s(vp_, voffV[i_], lv_ + i_ * 1024);                   \
  } while (0)

  for (int half = 0; half < 2; ++half) {
    const int qb = half == 0 ? (15 - pi) : pi;
    const int R0 = qb * 128;
    const int qrow = R0 + 32 * w4 + r32;
    bf16x8 qr[8];
#pragma unroll
    for (int d0 = 0; d0 < 8; ++d0) qr[d0] = *(const bf16x8*)(Qh + (size_t)qrow * HD + d0 * 16 + hh * 8);

    float m_run = -1e30f, l_run = 0.f;
    f32x16 o[4];
#pragma unroll
    for (int c = 0; c < 4; ++c)
#pragma unroll
      for (int i = 0; i < 16; ++i) o[c][i] = 0.f;
    f32x16 p0, p1;
#pragma unroll
    for (int i = 0; i < 16; ++i) { p0[i] = 0.f; p1[i] = 0.f; }

#define A_QK(st_) do {                                                                                           \
      const char* kb_ = smem + ((st_) & 1) * 32768 + grp * 16384;                                                \
      _Pragma("unroll") for (int i_ = 0; i_ < 16; ++i_) { p0[i_] = 0.f; p1[i_] = 0.f; }                          \
      bf16x8 kf_[8];                                                                                             \
      _Pragma("unroll") for (int d0 = 0; d0 < 8; ++d0) kf_[d0] = *(const bf16x8*)(kb_ + k_lane[d0]);             \
      _Pragma("unroll") for (int d0 = 0; d0 < 8; ++d0) p0 = MFMA32(kf_[d0], qr[d0], p0);                         \
      _Pragma("unroll") for (int d0 = 0; d0 < 8; ++d0) kf_[d0] = *(const bf16x8*)(kb_ + k_lane[d0] + 8192);      \
      _Pragma("unroll") for (int d0 = 0; d0 < 8; ++d0) p1 = MFMA32(kf_[d0], qr[d0], p1);                         \
    } while (0)
#define A_SMPV(st_) do {                                                                                         \
      const int key0_ = (2 * (st_) + grp) * 64;                                                                  \
      const char* vb_ = smem + 65536 + ((st_) % 3) * 32768 + grp * 16384;                                        \
      if (key0_ + 63 > R0 + 32 * w4) {                                                                           \
        const int dq_ = qrow - key0_ - 4 * hh;                                                                   \
        _Pragma("unroll") for (int r = 0; r < 16; ++r) {                                                         \
          const int c_ = (r & 3) + 8 * (r >> 2);                                                                 \
          if (c_ > dq_) p0[r] = -INFINITY;                                                                       \
          if (c_ + 32 > dq_) p1[r] = -INFINITY;                                                                  \
        }                                                                                                        \
      }                                                                                                          \
      float pmax_ = p0[0];                                                                                       \
      _Pragma("unroll") for (int r = 1; r < 16; ++r) pmax_ = fmaxf(pmax_, p0[r]);                                \
      _Pragma("unroll") for (int r = 0; r < 16; ++r) pmax_ = fmaxf(pmax_, p1[r]);                                \
      pmax_ = fmaxf(pmax_, __shfl_xor(pmax_, 32));                                                               \
      const float m_new_ = fmaxf(m_run, pmax_);                                                                  \
      const float alpha_ = __builtin_amdgcn_exp2f((m_run - m_new_) * CS);                                        \
      m_run = m_new_;                                                                                            \
      const float mc_ = -m_new_ * CS;                                                                            \
      float ps_ = 0.f;                                                                                           \
      _Pragma("unroll") for (int r = 0; r < 16; ++r) { p0[r] = __builtin_amdgcn_exp2f(fmaf(p0[r], CS, mc_)); ps_ += p0[r]; } \
      _Pragma("unroll") for (int r = 0; r < 16; ++r) { p1[r] = __builtin_amdgcn_exp2f(fmaf(p1[r], CS, mc_)); ps_ += p1[r]; } \
      l_run = l_run * alpha_ + ps_;                                                                              \
      if (__any(alpha_ < 1.f)) {                                                                                 \
        _Pragma("unroll") for (int c = 0; c < 4; ++c)                                                            \
          _Pragma("unroll") for (int i_ = 0; i_ < 16; ++i_) o[c][i_] *= alpha_;                                  \
      }                                                                                                          \
      bf16x8 pf_[4];                                                                                             \
      { u32x4 t_;                                                                                                \
        t_[0] = pk2(p0[0], p0[1]); t_[1] = pk2(p0[2], p0[3]); t_[2] = pk2(p0[4], p0[5]); t_[3] = pk2(p0[6], p0[7]); \
        pf_[0] = __builtin_bit_cast(bf16x8, t_);                                                                 \
        t_[0] = pk2(p0[8], p0[9]); t_[1] = pk2(p0[10], p0[11]); t_[2] = pk2(p0[12], p0[13]); t_[3] = pk2(p0[14], p0[15]); \
        pf_[1] = __builtin_bit_cast(bf16x8, t_);                                                                 \
        t_[0] = pk2(p1[0], p1[1]); t_[1] = pk2(p1[2], p1[3]); t_[2] = pk2(p1[4], p1[5]); t_[3] = pk2(p1[6], p1[7]); \
        pf_[2] = __builtin_bit_cast(bf16x8, t_);                                                                 \
        t_[0] = pk2(p1[8], p1[9]); t_[1] = pk2(p1[10], p1[11]); t_[2] = pk2(p1[12], p1[13]); t_[3] = pk2(p1[14], p1[15]); \
        pf_[3] = __builtin_bit_cast(bf16x8, t_); }                                                               \
      _Pragma("unroll") for (int c = 0; c < 4; ++c) {                                                            \
        bf16x8 vf_[4];                                                                                           \
        _Pragma("unroll") for (int s_ = 0; s_ < 4; ++s_) {                                                       \
          const char* pp_ = vb_ + v_lane + (4 * s_) * 1024 + c * 256;                                            \
          vf_[s_] = cat8(tr_read(pp_), tr_read(pp_ + 2048));                                                     \
        }                                                                                                        \
        _Pragma("unroll") for (int s_ = 0; s_ < 4; ++s_) o[c] = MFMA32(vf_[s_], pf_[s_], o[c]);                  \
      }                                                                                                          \
    } while (0)
#define A_ACT(st_) ((2 * (st_) + grp) * 64 <= R0 + 32 * w4 + 31)

    const int nst = qb + 1;
    A_STAGE(0);
    for (int st = 0; st < nst; ++st) {
      WAIT_VM(0);
      WAIT_LGKM0();
      BARRIER();
      if (st + 1 < nst) A_STAGE(st + 1);
      if (A_ACT(st)) { A_QK(st); A_SMPV(st); }
    }
    WAIT_LGKM0();
    BARRIER();

    float* mg = (float*)smem;
    if (grp == 1) {
      float* dst = mg + w4 * (66 * 64);
#pragma unroll
      for (int c = 0; c < 4; ++c)
#pragma unroll
        for (int i = 0; i < 16; ++i) dst[(c * 16 + i) * 64 + lane] = o[c][i];
      dst[64 * 64 + lane] = m_run;
      dst[65 * 64 + lane] = l_run;
    }
    __syncthreads();
    if (grp == 0) {
      const float* src = mg + w4 * (66 * 64);
      const float m1 = src[64 * 64 + lane], l1 = src[65 * 64 + lane];
      const float mm = fmaxf(m_run, m1);
      const float a0 = __builtin_amdgcn_exp2f((m_run - mm) * CS), a1 = __builtin_amdgcn_exp2f((m1 - mm) * CS);
      float lt = l_run * a0 + l1 * a1;
      lt += __shfl_xor(lt, 32);
      const float inv = 1.f / lt;
      u16* op = CTX + (size_t)qrow * HD + head * HDIM;
#pragma unroll
      for (int c = 0; c < 4; ++c) {
#pragma unroll
        for (int g = 0; g < 4; ++g) {
          float v[4];
#pragma unroll
          for (int e = 0; e < 4; ++e) v[e] = (o[c][4 * g + e] * a0 + src[(c * 16 + 4 * g + e) * 64 + lane] * a1) * inv;
          u32x2 w;
          w[0] = pk2(v[0], v[1]); w[1] = pk2(v[2], v[3]);
          *(u32x2*)(op + 32 * c + 8 * g + 4 * hh) = w;
        }
      }
    }
    __syncthreads();
  }
}

constexpr int B_OFF_V = 2 * SEQ * 16;
constexpr int B_OFF_ONES = B_OFF_V + SEQ * 32;
constexpr int B_OFF_MX = B_OFF_ONES + 256;
constexpr int B_OFF_O = B_OFF_MX + 8 * 2 * 32 * 4;
constexpr int B_LDS = B_OFF_O + 8 * 9 * 64 * 4;
constexpr float SCALE_B = 0.25f;

__global__ __launch_bounds__(1024, 1) void attnB_kernel(const u16* __restrict__ QB, const u16* __restrict__ KB,
                                                        const u16* __restrict__ VB, u16* __restrict__ OB) {
  extern __shared__ __attribute__((aligned(16))) char smem[];
  char* sk = smem;
  char* sv = smem + B_OFF_V;
  const int tid = threadIdx.x, wid = tid >> 6, lane = tid & 63, r32 = lane & 31, hh = lane >> 5;
  constexpr float CS = SCALE_B * LOG2E;
  for (int i = tid; i < SEQ * 2; i += 1024) {
    const int key = i >> 1, h = i & 1;
    *(u32x4*)(sk + h * (SEQ * 16) + key * 16) = *(const u32x4*)(KB + (size_t)key * 16 + h * 8);
    *(u32x4*)(sv + i * 16) = *(const u32x4*)(VB + (size_t)i * 8);
  }
  if (tid < 64) *(unsigned*)(smem + B_OFF_ONES + tid * 4) = 0x3f803f80u;
  __syncthreads();

  const int task = wid & 7, kh = wid >> 3;
  const int gw = blockIdx.x * 8 + task;
  const int head = gw & 31, rb = gw >> 5;
  const int q = rb * 32 + r32;
  const bf16x8 qf = *(const bf16x8*)(QB + (size_t)q * DM + head * 16 + hh * 8);
  const char* kp = sk + hh * (SEQ * 16) + (kh * 1024 + r32) * 16;
  const int q4 = (lane & 15) >> 2, p4 = lane & 3;
  const char* vp = ((lane >> 4) & 1) ? (smem + B_OFF_ONES + q4 * 32 + p4 * 8)
                                     : (sv + (kh * 1024 + 4 * hh + q4) * 32 + p4 * 8);
  const int vstep = ((lane >> 4) & 1) ? 0 : 32 * 32;
  const int vt8 = ((lane >> 4) & 1) ? 0 : 8 * 32;
  const int vs16 = ((lane >> 4) & 1) ? 0 : 16 * 32;

  f32x16 z;
#pragma unroll
  for (int i = 0; i < 16; ++i) z[i] = 0.f;
  float mx = -INFINITY;
#pragma unroll 2
  for (int T = 0; T < 32; T += 2) {
    const bf16x8 kf0 = *(const bf16x8*)(kp + T * 512);
    const bf16x8 kf1 = *(const bf16x8*)(kp + T * 512 + 512);
    const f32x16 s0 = MFMA32(kf0, qf, z);
    const f32x16 s1 = MFMA32(kf1, qf, z);
#pragma unroll
    for (int r = 0; r < 16; ++r) mx = fmaxf(mx, fmaxf(s0[r], s1[r]));
  }
  mx = fmaxf(mx, __shfl_xor(mx, 32));
  float* smx = (float*)(smem + B_OFF_MX) + task * 64;
  if (hh == 0) smx[kh * 32 + r32] = mx;
  __syncthreads();
  mx = fmaxf(smx[r32], smx[32 + r32]);
  const float mc = -mx * CS;
  f32x16 o;
#pragma unroll
  for (int i = 0; i < 16; ++i) o[i] = 0.f;
#pragma unroll 2
  for (int T = 0; T < 32; ++T) {
    const bf16x8 kf = *(const bf16x8*)(kp + T * 512);
    const char* v0 = vp + T * vstep;
    const bf16x8 va = cat8(tr_read(v0), tr_read(v0 + vt8));
    const bf16x8 vb = cat8(tr_read(v0 + vs16), tr_read(v0 + vs16 + vt8));
    f32x16 sc = MFMA32(kf, qf, z);
#pragma unroll
    for (int r = 0; r < 16; ++r) sc[r] = __builtin_amdgcn_exp2f(fmaf(sc[r], CS, mc));
    u32x4 t0, t1;
    t0[0] = pk2(sc[0], sc[1]); t0[1] = pk2(sc[2], sc[3]); t0[2] = pk2(sc[4], sc[5]); t0[3] = pk2(sc[6], sc[7]);
    t1[0] = pk2(sc[8], sc[9]); t1[1] = pk2(sc[10], sc[11]); t1[2] = pk2(sc[12], sc[13]); t1[3] = pk2(sc[14], sc[15]);
    o = MFMA32(va, __builtin_bit_cast(bf16x8, t0), o);
    o = MFMA32(vb, __builtin_bit_cast(bf16x8, t1), o);
  }
  float* so = (float*)(smem + B_OFF_O) + task * (9 * 64);
  if (kh == 1) {
#pragma unroll
    for (int i = 0; i < 9; ++i) so[i * 64 + lane] = o[i];
  }
  __syncthreads();
  if (kh == 0) {
#pragma unroll
    for (int i = 0; i < 9; ++i) o[i] += so[i * 64 + lane];
    const float inv = 1.f / o[8];
    u16* op = OB + (size_t)q * DM + head * 16;
    u32x2 w0, w1;
    w0[0] = pk2(o[0] * inv, o[1] * inv); w0[1] = pk2(o[2] * inv, o[3] * inv);
    w1[0] = pk2(o[4] * inv, o[5] * inv); w1[1] = pk2(o[6] * inv, o[7] * inv);
    *(u32x2*)(op + 4 * hh) = w0;
    *(u32x2*)(op + 8 + 4 * hh) = w1;
  }
}

constexpr int OP_STAGE = 32768;
constexpr int OP_NST = 4;
constexpr int OP_LDS = OP_NST * OP_STAGE;

__global__ __launch_bounds__(512, 1) void outproj_kernel(const u16* __restrict__ CTX, const u16* __restrict__ wo,
                                                         const u16* __restrict__ OB, const u16* __restrict__ bwo,
                                                         const float* __restrict__ gate, u16* __restrict__ out) {
  extern __shared__ __attribute__((aligned(16))) char smem[];
  const int tid = threadIdx.x, lane = tid & 63, r32 = lane & 31, hh = lane >> 5;
  const int wid = __builtin_amdgcn_readfirstlane(tid >> 6);
  const int grp = wid >> 2, w4 = wid & 3, wm = w4 & 1, wn = w4 >> 1;
  const int tm = (blockIdx.x & 7) * 4 + ((blockIdx.x >> 3) & 3), tn = blockIdx.x >> 5;
  const int m0 = tm * 64, n0 = tn * 64;
  const unsigned sbase = lds_addr(smem);

  const int a_rowl = lane >> 3;
  const int a_ch0 = (lane & 7) ^ ((lane >> 4) & 7);
  const int a_ch1 = (lane & 7) ^ ((4 + (lane >> 4)) & 7);
  const int w_st = lane >> 4, w_kr = (lane >> 2) & 3, w_c4 = lane & 3;

  const int nsteps = 32 + 4;
  unsigned voffA[2], voffW[2];
  const char* st_A = (const char*)(CTX + (size_t)m0 * HD + grp * 2048);
  const char* st_W = (const char*)(wo + (size_t)(grp * 2048) * DM + n0);
  int st_s = 0;
#pragma unroll
  for (int i = 0; i < 2; ++i) {
    const int j = w4 * 2 + i;
    voffA[i] = (unsigned)((8 * j + a_rowl) * HD + ((i & 1) ? a_ch1 : a_ch0) * 8) * 2u;
    voffW[i] = (unsigned)((8 * j + 4 * (w_st >> 1) + w_kr) * DM + 32 * (w_st & 1) + 8 * w_c4) * 2u;
  }
#define OP_STAGE_ISSUE() do {                                                                                    \
    if (st_s == 32) {                                                                                            \
      st_A = (const char*)(OB + (size_t)m0 * DM + grp * 256);                                                    \
      st_W = (const char*)(bwo + (size_t)(grp * 256) * DM + n0);                                                 \
      _Pragma("unroll") for (int i_ = 0; i_ < 2; ++i_)                                                           \
        voffA[i_] = (unsigned)((8 * (w4 * 2 + i_) + a_rowl) * DM + ((i_ & 1) ? a_ch1 : a_ch0) * 8) * 2u;         \
    }                                                                                                            \
    const unsigned lb_ = sbase + (st_s & 3) * OP_STAGE + grp * 16384;                                            \
    _Pragma("unroll") for (int i_ = 0; i_ < 2; ++i_) glds16s(st_A, voffA[i_], lb_ + (w4 * 2 + i_) * 1024);       \
    _Pragma("unroll") for (int i_ = 0; i_ < 2; ++i_) glds16s(st_W, voffW[i_], lb_ + 8192 + (w4 * 2 + i_) * 1024); \
    st_A += 128; st_W += (size_t)64 * DM * 2; ++st_s;                                                            \
  } while (0)

  const int blk = (lane >> 4) & 1, q4 = (lane & 15) >> 2, p4 = lane & 3;
  const int w_lane = (hh * 2) * 512 + wn * 256 + q4 * 64 + blk * 32 + p4 * 8;
  const int a_row = wm * 32 + r32;

  f32x16 acc_a, acc_b;
#pragma unroll
  for (int i = 0; i < 16; ++i) { acc_a[i] = 0.f; acc_b[i] = 0.f; }

  OP_STAGE_ISSUE(); OP_STAGE_ISSUE(); OP_STAGE_ISSUE();
  for (int step = 0; step < nsteps; ++step) {
    if (step + 2 < nsteps) { WAIT_VM(8); } else if (step + 1 < nsteps) { WAIT_VM(4); } else { WAIT_VM(0); }
    WAIT_LGKM0();
    BARRIER();
    if (step + 3 < nsteps) OP_STAGE_ISSUE();
    const char* sa = smem + (step & 3) * OP_STAGE + grp * 16384;
    const char* sw = sa + 8192;
    if (step < 32) {
#pragma unroll
      for (int ks = 0; ks < 4; ++ks) {
        const char* p = sw + w_lane + (4 * ks) * 512;
        const bf16x8 wf = cat8(tr_read(p), tr_read(p + 512));
        const bf16x8 af = *(const bf16x8*)(sa + a_off(a_row, 2 * ks + hh));
        acc_a = MFMA32(wf, af, acc_a);
      }
    } else {
#pragma unroll
      for (int ks = 0; ks < 4; ++ks) {
        const char* p = sw + w_lane + (4 * ks) * 512;
        const bf16x8 wf = cat8(tr_read(p), tr_read(p + 512));
        const bf16x8 af = *(const bf16x8*)(sa + a_off(a_row, 2 * ks + hh));
        acc_b = MFMA32(wf, af, acc_b);
      }
    }
  }
  WAIT_LGKM0();
  BARRIER();
  float* mg = (float*)smem;
  if (grp == 1) {
    float* dst = mg + w4 * (32 * 64);
#pragma unroll
    for (int i = 0; i < 16; ++i) { dst[i * 64 + lane] = acc_a[i]; dst[(16 + i) * 64 + lane] = acc_b[i]; }
  }
  __syncthreads();
  if (grp == 0) {
    const float* s = mg + w4 * (32 * 64);
    const int m = m0 + wm * 32 + r32;
    const float g = gate[m];
    const float omg = rbf(1.f - g);
#pragma unroll
    for (int gq = 0; gq < 4; ++gq) {
      float v[4];
#pragma unroll
      for (int e = 0; e < 4; ++e) {
        const int i = 4 * gq + e;
        const float a = rbf(acc_a[i] + s[i * 64 + lane]);
        const float b = rbf(acc_b[i] + s[(16 + i) * 64 + lane]);
        v[e] = rbf(g * a) + rbf(omg * b);
      }
      u32x2 w;
      w[0] = pk2(v[0], v[1]); w[1] = pk2(v[2], v[3]);
      *(u32x2*)(out + (size_t)m * DM + n0 + wn * 32 + 8 * gq + 4 * hh) = w;
    }
  }
}

extern "C" void kernel_launch(void* const* d_in, const int* in_sizes, int n_in,
                              void* d_out, int out_size, void* d_ws, size_t ws_size,
                              hipStream_t stream) {
  const u16* A_x = (const u16*)d_in[0];
  const u16* A_ckv = (const u16*)d_in[1];
  const u16* A_wq = (const u16*)d_in[2];
  const u16* A_wuk = (const u16*)d_in[3];
  const u16* A_wuv = (const u16*)d_in[4];
  const u16* A_wo = (const u16*)d_in[5];
  const u16* B_wq = (const u16*)d_in[7];
  const u16* B_wk = (const u16*)d_in[8];
  const u16* B_wv = (const u16*)d_in[9];
  const u16* B_wo = (const u16*)d_in[10];
  const u16* gate_w = (const u16*)d_in[11];
  char* ws = (char*)d_ws;
  u16* QA = (u16*)(ws + WS_QA);
  u16* KA = (u16*)(ws + WS_KA);
  u16* VA = (u16*)(ws + WS_VA);
  u16* CTX = (u16*)(ws + WS_CTX);
  u16* QB = (u16*)(ws + WS_QB);
  u16* OB = (u16*)(ws + WS_OB);
  u16* KB = (u16*)(ws + WS_KB);
  u16* VB = (u16*)(ws + WS_VB);
  float* gate = (float*)(ws + WS_GATE);

  hipFuncSetAttribute((const void*)attnA_kernel, hipFuncAttributeMaxDynamicSharedMemorySize, AA_LDS);
  hipFuncSetAttribute((const void*)attnB_kernel, hipFuncAttributeMaxDynamicSharedMemorySize, B_LDS);

  hipFuncSetAttribute((const void*)proj_kernel, hipFuncAttributeMaxDynamicSharedMemorySize, PJ_LDS);
  hipFuncSetAttribute((const void*)outproj_kernel, hipFuncAttributeMaxDynamicSharedMemorySize, OP_LDS);
  proj_kernel<<<256, 512, PJ_LDS, stream>>>(A_x, A_ckv, A_wq, A_wuk, A_wuv, B_wq, B_wk, B_wv, gate_w,
                                          QA, KA, VA, QB, KB, VB, gate);
  attnA_kernel<<<256, 512, AA_LDS, stream>>>(QA, KA, VA, CTX);
  attnB_kernel<<<256, 1024, B_LDS, stream>>>(QB, KB, VB, OB);
  outproj_kernel<<<256, 512, OP_LDS, stream>>>(CTX, A_wo, OB, B_wo, gate, (u16*)d_out);
}
```
